```python
import torch

def _softmax(x, axis=-1):
    return torch.softmax(x, dim=axis)

def _gelu(x):
    return torch.nn.functional.gelu(x, approximate='tanh')

def _silu(x):
    return torch.nn.functional.silu(x)

def _sigmoid(x):
    return torch.sigmoid(x)

def _layernorm(x, axis=-1, eps=1e-05):
    m = x.mean(dim=axis, keepdim=True)
    v = x.var(dim=axis, keepdim=True, unbiased=False)
    return (x - m) / torch.sqrt(v + eps)

def _scatter_add(tgt, idx, src, axis=0):
    return tgt.clone().index_add_(axis, idx, src)

def _segment_sum(src, seg, n):
    out = torch.zeros((n,) + src.shape[1:], dtype=src.dtype)
    return out.index_add_(0, seg, src)

def _pad(x, pw):
    flat = tuple((v for pair in reversed(pw) for v in pair))
    return torch.nn.functional.pad(x, flat)
kernel_name = 'standalone__tree_speculative_verify_tree_attention_over_candidate_token_tree__sampling'

def setup_inputs():

    _w = lambda a: (lambda t: t.to(torch.bfloat16) if t.is_floating_point() else t)(torch.as_tensor(a))
    B, Hq, Hkv, T, C, D = (8, 32, 16, 4, 8192, 128)
    tree_mask = torch.zeros((B, T, T), dtype=float)
    for b in range(B):
        parent = torch.zeros(T, dtype=torch.int64)
        for i in range(1, T):
            parent[i] = int(torch.randint(0, i, (1,)).item())
        for i in range(T):
            j = i
            tree_mask[b, i, j] = 1.0
            while j > 0:
                j = parent[j]
                tree_mask[b, i, j] = 1.0
    return {'q': _w((lambda *s: torch.randn(*s, dtype=torch.bfloat16) * 0.8)(B, Hq, T, D)), 'k_cache': _w((lambda *s: torch.randn(*s, dtype=torch.bfloat16) * 0.8)(B, Hkv, C, D)), 'v_cache': _w((lambda *s: torch.randn(*s, dtype=torch.bfloat16) * 0.05)(B, Hkv, C, D)), 'k_tree': _w((lambda *s: torch.randn(*s, dtype=torch.bfloat16) * 0.8)(B, Hkv, T, D)), 'v_tree': _w((lambda *s: torch.randn(*s, dtype=torch.bfloat16) * 0.05)(B, Hkv, T, D)), 'tree_mask': _w(tree_mask)}

def reference(q, k_cache, v_cache, k_tree, v_tree, tree_mask):
    B, Hq, T, D = q.shape
    Hkv, C = (k_cache.shape[1], k_cache.shape[2])
    rep = Hq // Hkv
    kc = (lambda x, n, axis: torch.repeat_interleave(x, n, dim=axis))(k_cache, rep, axis=1)
    vc = (lambda x, n, axis: torch.repeat_interleave(x, n, dim=axis))(v_cache, rep, axis=1)
    kt = (lambda x, n, axis: torch.repeat_interleave(x, n, dim=axis))(k_tree, rep, axis=1)
    vt = (lambda x, n, axis: torch.repeat_interleave(x, n, dim=axis))(v_tree, rep, axis=1)
    k = torch.concatenate([kc, kt], axis=2)
    v = torch.concatenate([vc, vt], axis=2)
    scale = D ** (-0.5)
    scores = torch.einsum('bhqd,bhkd->bhqk', q, k) * scale
    ctx_mask = torch.ones((B, 1, T, C), dtype=scores.dtype)
    tm = tree_mask[:, None, :, :].clone()
    full_mask = torch.concatenate([ctx_mask, tm], axis=-1)
    scores = scores + (1.0 - full_mask) * -1000000000.0
    p = _softmax(scores, axis=-1)
    return torch.einsum('bhqk,bhkd->bhqd', p, v)
```

```cpp
#include <hip/hip_runtime.h>
#include <hip/hip_bf16.h>
#include <stdint.h>

namespace {

constexpr int B_ = 8, HQ = 32, HKV = 16, T_ = 4, C_ = 8192, D_ = 128;
constexpr int REP = HQ / HKV;
constexpr int M_ = REP * T_;
constexpr int NBH = B_ * HKV;

#define K_WAVES 4
#define K_NSPLIT 4
#define K_NT 1
constexpr int WAVES = K_WAVES;
constexpr int NSPLIT = K_NSPLIT;
constexpr int NTHR = WAVES * 64;
constexpr int KEYS_PER_WAVE = C_ / (NSPLIT * WAVES);
constexpr int TILE = 32;
constexpr int NTILES = KEYS_PER_WAVE / TILE;
static_assert(NTILES >= 2 && NTILES % 2 == 0, "even tile count");

constexpr float SCALE = 0.088388347648318440f;
constexpr float LOG2E = 1.4426950408889634f;
constexpr float C2 = SCALE * LOG2E;

constexpr size_t WS_PML = 0;
constexpr size_t WS_PO = 65536;
constexpr size_t WS_END = WS_PO + (size_t)NBH * NSPLIT * M_ * D_ * 4;

typedef __attribute__((ext_vector_type(8))) __bf16 bf16x8;
typedef __attribute__((ext_vector_type(2))) __bf16 bf16x2;
typedef __attribute__((ext_vector_type(2))) float f32x2;
typedef __attribute__((ext_vector_type(4))) float f32x4;
typedef __attribute__((ext_vector_type(4))) unsigned int u32x4;

__device__ __forceinline__ u32x4 ld_stream(const char* p) {
  return __builtin_nontemporal_load(reinterpret_cast<const u32x4*>(p));
}

__device__ __forceinline__ f32x4 mfma16(u32x4 a, u32x4 b, f32x4 c) {
  return __builtin_amdgcn_mfma_f32_16x16x32_bf16(__builtin_bit_cast(bf16x8, a), __builtin_bit_cast(bf16x8, b), c, 0, 0, 0);
}

__device__ __forceinline__ unsigned pack_bf16(float lo, float hi) {
  f32x2 v = {lo, hi};
  return __builtin_bit_cast(unsigned, __builtin_convertvector(v, bf16x2));
}

__device__ __forceinline__ float xg_max(float x) {
  auto s = __builtin_amdgcn_permlane16_swap(__float_as_uint(x), __float_as_uint(x), false, false);
  x = fmaxf(__uint_as_float(s[0]), __uint_as_float(s[1]));
  auto t = __builtin_amdgcn_permlane32_swap(__float_as_uint(x), __float_as_uint(x), false, false);
  return fmaxf(__uint_as_float(t[0]), __uint_as_float(t[1]));
}
__device__ __forceinline__ float xg_sum(float x) {
  auto s = __builtin_amdgcn_permlane16_swap(__float_as_uint(x), __float_as_uint(x), false, false);
  x = __uint_as_float(s[0]) + __uint_as_float(s[1]);
  auto t = __builtin_amdgcn_permlane32_swap(__float_as_uint(x), __float_as_uint(x), false, false);
  return __uint_as_float(t[0]) + __uint_as_float(t[1]);
}

struct Tile {
  u32x4 k[8];
  u32x4 v[8];
};

__device__ __forceinline__ void load_tile(Tile& t, const char* kp, const char* vp) {
#pragma unroll
  for (int i = 0; i < 8; ++i) t.k[i] = ld_stream(kp + (i >> 2) * 4096 + (i & 3) * 64);
#pragma unroll
  for (int j = 0; j < 8; ++j) t.v[j] = ld_stream(vp + (j >> 2) * 4096 + (j & 3) * 256);
}

__device__ __forceinline__ void compute_tile(const Tile& t, const u32x4 (&qf)[4], float& m, float& l, f32x4 (&acc)[8]) {
  f32x4 s0 = {0.f, 0.f, 0.f, 0.f}, s1 = {0.f, 0.f, 0.f, 0.f};
#pragma unroll
  for (int ks = 0; ks < 4; ++ks) {
    s0 = mfma16(t.k[ks], qf[ks], s0);
    s1 = mfma16(t.k[4 + ks], qf[ks], s1);
  }
  float mx = fmaxf(fmaxf(fmaxf(s0[0], s0[1]), fmaxf(s0[2], s0[3])), fmaxf(fmaxf(s1[0], s1[1]), fmaxf(s1[2], s1[3])));
  mx = xg_max(mx);
  if (__builtin_amdgcn_ballot_w64(mx > m) != 0ull) {
    const float mn = fmaxf(m, mx);
    const float alpha = __builtin_amdgcn_exp2f((m - mn) * C2);
    m = mn;
    l *= alpha;
#pragma unroll
    for (int i = 0; i < 8; ++i) acc[i] *= alpha;
  }
  const float mc = m * C2;
  float p0[4], p1[4];
#pragma unroll
  for (int r = 0; r < 4; ++r) {
    p0[r] = __builtin_amdgcn_exp2f(fmaf(s0[r], C2, -mc));
    p1[r] = __builtin_amdgcn_exp2f(fmaf(s1[r], C2, -mc));
  }
  l += ((p0[0] + p0[1]) + (p0[2] + p0[3])) + ((p1[0] + p1[1]) + (p1[2] + p1[3]));
  u32x4 pf = {pack_bf16(p0[0], p0[1]), pack_bf16(p0[2], p0[3]), pack_bf16(p1[0], p1[1]), pack_bf16(p1[2], p1[3])};
#pragma unroll
  for (int i = 0; i < 8; ++i) {
    const unsigned sel = (i & 1) ? 0x07060302u : 0x05040100u;
    u32x4 vt;
#pragma unroll
    for (int w = 0; w < 4; ++w) vt[w] = __builtin_amdgcn_perm(t.v[2 * w + 1][i >> 1], t.v[2 * w][i >> 1], sel);
    acc[i] = mfma16(vt, pf, acc[i]);
  }
}

constexpr int LROW = D_ + 4;
constexpr int LDS_O = 0, LDS_ML = WAVES * M_ * LROW;
constexpr int LDS_FLOATS = LDS_ML + WAVES * M_ * 2;

__global__ void __launch_bounds__(NTHR, 2)
attn_split_kernel(const __hip_bfloat16* __restrict__ q, const __hip_bfloat16* __restrict__ kc,
                  const __hip_bfloat16* __restrict__ vc, float* __restrict__ pml, float* __restrict__ po) {
  __shared__ __attribute__((aligned(16))) float lds[LDS_FLOATS];
  const int tid = threadIdx.x, lane = tid & 63;
  const int wave = __builtin_amdgcn_readfirstlane(tid >> 6);
  const int c = lane & 15, g = lane >> 4;
  const int bh = blockIdx.x / NSPLIT, split = blockIdx.x % NSPLIT;
  const int key0 = (split * WAVES + wave) * KEYS_PER_WAVE;

  const char* kbase = reinterpret_cast<const char*>(kc) + ((size_t)bh * C_ + key0) * (D_ * 2);
  const char* vbase = reinterpret_cast<const char*>(vc) + ((size_t)bh * C_ + key0) * (D_ * 2);
  const char* kp = kbase + c * 256 + g * 16;
  const char* vp = vbase + g * 1024 + c * 16;

  Tile ta, tb;
  load_tile(ta, kp, vp);

  u32x4 qf[4];
  {
    const char* qp = reinterpret_cast<const char*>(q) + ((size_t)bh * M_ + (c & 7)) * (D_ * 2) + g * 16;
#pragma unroll
    for (int ks = 0; ks < 4; ++ks) {
      u32x4 x = *reinterpret_cast<const u32x4*>(qp + ks * 64);
      qf[ks] = (c < M_) ? x : u32x4{0u, 0u, 0u, 0u};
    }
  }

  float m = -INFINITY, l = 0.f;
  f32x4 acc[8];
#pragma unroll
  for (int i = 0; i < 8; ++i) acc[i] = f32x4{0.f, 0.f, 0.f, 0.f};

  for (int it = 0; it < NTILES; it += 2) {
    load_tile(tb, kp + (it + 1) * (TILE * 256), vp + (it + 1) * (TILE * 256));
    compute_tile(ta, qf, m, l, acc);
    if (it + 2 < NTILES) load_tile(ta, kp + (it + 2) * (TILE * 256), vp + (it + 2) * (TILE * 256));
    compute_tile(tb, qf, m, l, acc);
  }

  l = xg_sum(l);
  if (c < M_) {
    float* o = lds + LDS_O + (wave * M_ + c) * LROW + 32 * g;
#pragma unroll
    for (int r = 0; r < 4; ++r) {
      *reinterpret_cast<f32x4*>(o + 8 * r) = f32x4{acc[0][r], acc[1][r], acc[2][r], acc[3][r]};
      *reinterpret_cast<f32x4*>(o + 8 * r + 4) = f32x4{acc[4][r], acc[5][r], acc[6][r], acc[7][r]};
    }
    if (g == 0) {
      lds[LDS_ML + (wave * M_ + c) * 2] = m;
      lds[LDS_ML + (wave * M_ + c) * 2 + 1] = l;
    }
  }
  __syncthreads();
  for (int idx = tid; idx < M_ * (D_ / 4); idx += NTHR) {
    const int row = idx >> 5, d4 = (idx & 31) * 4;
    float M = -INFINITY;
#pragma unroll
    for (int w = 0; w < WAVES; ++w) M = fmaxf(M, lds[LDS_ML + (w * M_ + row) * 2]);
    float L = 0.f;
    f32x4 o = {0.f, 0.f, 0.f, 0.f};
#pragma unroll
    for (int w = 0; w < WAVES; ++w) {
      const float wt = __builtin_amdgcn_exp2f((lds[LDS_ML + (w * M_ + row) * 2] - M) * C2);
      L = fmaf(wt, lds[LDS_ML + (w * M_ + row) * 2 + 1], L);
      o += wt * *reinterpret_cast<const f32x4*>(lds + LDS_O + (w * M_ + row) * LROW + d4);
    }
    const size_t slot = ((size_t)bh * NSPLIT + split) * M_ + row;
    *reinterpret_cast<f32x4*>(po + slot * D_ + d4) = o;
    if ((idx & 31) == 0) *reinterpret_cast<f32x2*>(pml + slot * 2) = f32x2{M, L};
  }
}

__device__ __forceinline__ float wave_sum(float x) {
#pragma unroll
  for (int off = 32; off >= 1; off >>= 1) x += __shfl_xor(x, off, 64);
  return x;
}

__global__ void __launch_bounds__(256)
attn_combine_kernel(const __hip_bfloat16* __restrict__ q, const __hip_bfloat16* __restrict__ kt,
                    const __hip_bfloat16* __restrict__ vt, const __hip_bfloat16* __restrict__ mask,
                    const float* __restrict__ pml, const float* __restrict__ po, __hip_bfloat16* __restrict__ out) {
  const int lane = threadIdx.x & 63;
  const int row = blockIdx.x * 4 + (threadIdx.x >> 6);
  const int t = row & 3, bhq = row >> 2, b = bhq / HQ, h = bhq % HQ;
  const int kvh = h / REP, bh = b * HKV + kvh, cq = (h % REP) * T_ + t;

  float pm[NSPLIT], pl[NSPLIT];
  f32x2 pov[NSPLIT];
#pragma unroll
  for (int s = 0; s < NSPLIT; ++s) {
    const size_t slot = ((size_t)bh * NSPLIT + s) * M_ + cq;
    const f32x2 ml = *reinterpret_cast<const f32x2*>(pml + slot * 2);
    pm[s] = ml[0];
    pl[s] = ml[1];
    pov[s] = *reinterpret_cast<const f32x2*>(po + slot * D_ + 2 * lane);
  }
  const unsigned qw = *reinterpret_cast<const unsigned*>(reinterpret_cast<const char*>(q) + ((size_t)row * D_ + 2 * lane) * 2);
  const float q0 = __uint_as_float(qw << 16), q1 = __uint_as_float(qw & 0xffff0000u);
  float z[T_], v0[T_], v1[T_];
#pragma unroll
  for (int j = 0; j < T_; ++j) {
    const size_t off = (((size_t)bh * T_ + j) * D_ + 2 * lane) * 2;
    const unsigned kw = *reinterpret_cast<const unsigned*>(reinterpret_cast<const char*>(kt) + off);
    const unsigned vw = *reinterpret_cast<const unsigned*>(reinterpret_cast<const char*>(vt) + off);
    v0[j] = __uint_as_float(vw << 16);
    v1[j] = __uint_as_float(vw & 0xffff0000u);
    float d = q0 * __uint_as_float(kw << 16) + q1 * __uint_as_float(kw & 0xffff0000u);
    d = wave_sum(d);
    const float mk = __bfloat162float(mask[(b * T_ + t) * T_ + j]);
    z[j] = (d * SCALE + (1.0f - mk) * -1000000000.0f) * LOG2E;
  }
  float M = -INFINITY;
#pragma unroll
  for (int s = 0; s < NSPLIT; ++s) { pm[s] *= C2; M = fmaxf(M, pm[s]); }
#pragma unroll
  for (int j = 0; j < T_; ++j) M = fmaxf(M, z[j]);
  float L = 0.f, o0 = 0.f, o1 = 0.f;
#pragma unroll
  for (int s = 0; s < NSPLIT; ++s) {
    const float w = __builtin_amdgcn_exp2f(pm[s] - M);
    L = fmaf(w, pl[s], L);
    o0 = fmaf(w, pov[s][0], o0);
    o1 = fmaf(w, pov[s][1], o1);
  }
#pragma unroll
  for (int j = 0; j < T_; ++j) {
    const float w = __builtin_amdgcn_exp2f(z[j] - M);
    L += w;
    o0 = fmaf(w, v0[j], o0);
    o1 = fmaf(w, v1[j], o1);
  }
  const float inv = 1.0f / L;
  *reinterpret_cast<unsigned*>(reinterpret_cast<char*>(out) + ((size_t)row * D_ + 2 * lane) * 2) = pack_bf16(o0 * inv, o1 * inv);
}

}

extern "C" void kernel_launch(void* const* d_in, const int* in_sizes, int n_in, void* d_out, int out_size, void* d_ws,
                              size_t ws_size, hipStream_t stream) {
  (void)in_sizes; (void)n_in; (void)out_size; (void)ws_size;
  const __hip_bfloat16* q = (const __hip_bfloat16*)d_in[0];
  const __hip_bfloat16* kc = (const __hip_bfloat16*)d_in[1];
  const __hip_bfloat16* vc = (const __hip_bfloat16*)d_in[2];
  const __hip_bfloat16* kt = (const __hip_bfloat16*)d_in[3];
  const __hip_bfloat16* vt = (const __hip_bfloat16*)d_in[4];
  const __hip_bfloat16* mask = (const __hip_bfloat16*)d_in[5];
  float* pml = (float*)((char*)d_ws + WS_PML);
  float* po = (float*)((char*)d_ws + WS_PO);
  hipLaunchKernelGGL(attn_split_kernel, dim3(NBH * NSPLIT), dim3(NTHR), 0, stream, q, kc, vc, pml, po);
  hipLaunchKernelGGL(attn_combine_kernel, dim3(B_ * HQ * T_ / 4), dim3(256), 0, stream, q, kt, vt, mask,
                     (const float*)pml, (const float*)po, (__hip_bfloat16*)d_out);
}
```

```cpp
#include <hip/hip_runtime.h>
#include <hip/hip_bf16.h>
#include <stdint.h>

namespace {

constexpr int B_ = 8, HQ = 32, HKV = 16, T_ = 4, C_ = 8192, D_ = 128;
constexpr int REP = HQ / HKV;
constexpr int M_ = REP * T_;
constexpr int NBH = B_ * HKV;

#define K_WAVES 2
#define K_NSPLIT 2
#define K_NT 1
#define K_ILV 2
constexpr int WAVES = K_WAVES;
constexpr int NSPLIT = K_NSPLIT;
constexpr int NTHR = WAVES * 64;
constexpr int KEYS_PER_WAVE = C_ / (NSPLIT * WAVES);
constexpr int TILE = 32;
constexpr int NTILES = KEYS_PER_WAVE / TILE;
#define K_NBUF 2
constexpr int NBUF = K_NBUF;
static_assert(NTILES >= NBUF && NTILES % NBUF == 0, "tile count");
#define K_SPLIT 1
#define K_MINW 1

constexpr float SCALE = 0.088388347648318440f;
constexpr float LOG2E = 1.4426950408889634f;
constexpr float C2 = SCALE * LOG2E;

constexpr size_t WS_PML = 0;
constexpr size_t WS_PO = 65536;
constexpr size_t WS_END = WS_PO + (size_t)NBH * NSPLIT * M_ * D_ * 4;

typedef __attribute__((ext_vector_type(8))) __bf16 bf16x8;
typedef __attribute__((ext_vector_type(2))) __bf16 bf16x2;
typedef __attribute__((ext_vector_type(2))) float f32x2;
typedef __attribute__((ext_vector_type(4))) float f32x4;
typedef __attribute__((ext_vector_type(4))) unsigned int u32x4;

__device__ __forceinline__ u32x4 ld_stream(const char* p) {
  return __builtin_nontemporal_load(reinterpret_cast<const u32x4*>(p));
}

__device__ __forceinline__ f32x4 mfma16(u32x4 a, u32x4 b, f32x4 c) {
  return __builtin_amdgcn_mfma_f32_16x16x32_bf16(__builtin_bit_cast(bf16x8, a), __builtin_bit_cast(bf16x8, b), c, 0, 0, 0);
}

__device__ __forceinline__ unsigned pack_bf16(float lo, float hi) {
  f32x2 v = {lo, hi};
  return __builtin_bit_cast(unsigned, __builtin_convertvector(v, bf16x2));
}

__device__ __forceinline__ float xg_max(float x) {
  auto s = __builtin_amdgcn_permlane16_swap(__float_as_uint(x), __float_as_uint(x), false, false);
  x = fmaxf(__uint_as_float(s[0]), __uint_as_float(s[1]));
  auto t = __builtin_amdgcn_permlane32_swap(__float_as_uint(x), __float_as_uint(x), false, false);
  return fmaxf(__uint_as_float(t[0]), __uint_as_float(t[1]));
}
__device__ __forceinline__ float xg_sum(float x) {
  auto s = __builtin_amdgcn_permlane16_swap(__float_as_uint(x), __float_as_uint(x), false, false);
  x = __uint_as_float(s[0]) + __uint_as_float(s[1]);
  auto t = __builtin_amdgcn_permlane32_swap(__float_as_uint(x), __float_as_uint(x), false, false);
  return __uint_as_float(t[0]) + __uint_as_float(t[1]);
}

struct KTile { u32x4 k[8]; };
struct VTile { u32x4 v[8]; };

__device__ __forceinline__ void load_k(KTile& t, const char* kp) {
#pragma unroll
  for (int i = 0; i < 8; ++i) t.k[i] = ld_stream(kp + (i >> 2) * 4096 + (i & 3) * 64);
}
__device__ __forceinline__ void load_v(VTile& t, const char* vp) {
#pragma unroll
  for (int j = 0; j < 8; ++j) t.v[j] = ld_stream(vp + (j >> 2) * 4096 + (j & 3) * 256);
}

__device__ __forceinline__ void qk_tile(const KTile& t, const u32x4 (&qf)[4], f32x4& s0, f32x4& s1) {
  s0 = f32x4{0.f, 0.f, 0.f, 0.f};
  s1 = f32x4{0.f, 0.f, 0.f, 0.f};
#pragma unroll
  for (int ks = 0; ks < 4; ++ks) {
    s0 = mfma16(t.k[ks], qf[ks], s0);
    s1 = mfma16(t.k[4 + ks], qf[ks], s1);
  }
}

__device__ __forceinline__ u32x4 softmax_tile(const f32x4& s0, const f32x4& s1, float& m, float& l, f32x4 (&acc)[8]) {
  float mx = fmaxf(fmaxf(fmaxf(s0[0], s0[1]), fmaxf(s0[2], s0[3])), fmaxf(fmaxf(s1[0], s1[1]), fmaxf(s1[2], s1[3])));
  mx = xg_max(mx);
  if (__builtin_amdgcn_ballot_w64(mx > m) != 0ull) {
    const float mn = fmaxf(m, mx);
    const float alpha = __builtin_amdgcn_exp2f((m - mn) * C2);
    m = mn;
    l *= alpha;
#pragma unroll
    for (int i = 0; i < 8; ++i) acc[i] *= alpha;
  }
  const float mc = m * C2;
  float p0[4], p1[4];
#pragma unroll
  for (int r = 0; r < 4; ++r) {
    p0[r] = __builtin_amdgcn_exp2f(fmaf(s0[r], C2, -mc));
    p1[r] = __builtin_amdgcn_exp2f(fmaf(s1[r], C2, -mc));
  }
  l += ((p0[0] + p0[1]) + (p0[2] + p0[3])) + ((p1[0] + p1[1]) + (p1[2] + p1[3]));
  return u32x4{pack_bf16(p0[0], p0[1]), pack_bf16(p0[2], p0[3]), pack_bf16(p1[0], p1[1]), pack_bf16(p1[2], p1[3])};
}

__device__ __forceinline__ void pv_tile(const VTile& t, const u32x4& pf, f32x4 (&acc)[8]) {
#pragma unroll
  for (int i = 0; i < 8; ++i) {
    const unsigned sel = (i & 1) ? 0x07060302u : 0x05040100u;
    u32x4 vt;
#pragma unroll
    for (int w = 0; w < 4; ++w) vt[w] = __builtin_amdgcn_perm(t.v[2 * w + 1][i >> 1], t.v[2 * w][i >> 1], sel);
    acc[i] = mfma16(vt, pf, acc[i]);
  }
}

constexpr int LROW = D_ + 4;
constexpr int LDS_O = 0, LDS_ML = WAVES * M_ * LROW;
constexpr int LDS_FLOATS = LDS_ML + WAVES * M_ * 2;

__global__ void __launch_bounds__(NTHR, K_MINW)
attn_split_kernel(const __hip_bfloat16* __restrict__ q, const __hip_bfloat16* __restrict__ kc,
                  const __hip_bfloat16* __restrict__ vc, float* __restrict__ pml, float* __restrict__ po) {
  __shared__ __attribute__((aligned(16))) float lds[LDS_FLOATS];
  const int tid = threadIdx.x, lane = tid & 63;
  const int wave = __builtin_amdgcn_readfirstlane(tid >> 6);
  const int c = lane & 15, g = lane >> 4;
  const int bh = blockIdx.x / NSPLIT, split = blockIdx.x % NSPLIT;
  const int key0 = (split * WAVES + wave) * TILE;
  constexpr int TSTRIDE = TILE * 256 * WAVES * NSPLIT;

  const char* kbase = reinterpret_cast<const char*>(kc) + ((size_t)bh * C_ + key0) * (D_ * 2);
  const char* vbase = reinterpret_cast<const char*>(vc) + ((size_t)bh * C_ + key0) * (D_ * 2);
  const char* kp = kbase + c * 256 + g * 16;
  const char* vp = vbase + g * 1024 + c * 16;

  KTile kb[NBUF];
  VTile vb[NBUF];
#pragma unroll
  for (int b = 0; b < NBUF; ++b) { load_k(kb[b], kp + b * TSTRIDE); load_v(vb[b], vp + b * TSTRIDE); }

  u32x4 qf[4];
  {
    const char* qp = reinterpret_cast<const char*>(q) + ((size_t)bh * M_ + (c & 7)) * (D_ * 2) + g * 16;
#pragma unroll
    for (int ks = 0; ks < 4; ++ks) {
      u32x4 x = *reinterpret_cast<const u32x4*>(qp + ks * 64);
      qf[ks] = (c < M_) ? x : u32x4{0u, 0u, 0u, 0u};
    }
  }

  float m = -INFINITY, l = 0.f;
  f32x4 acc[8];
#pragma unroll
  for (int i = 0; i < 8; ++i) acc[i] = f32x4{0.f, 0.f, 0.f, 0.f};

  f32x4 s0, s1;
  for (int it = 0; it < NTILES - NBUF; it += NBUF) {
#pragma unroll
    for (int b = 0; b < NBUF; ++b) {
      qk_tile(kb[b], qf, s0, s1);
      __builtin_amdgcn_sched_barrier(0);
      load_k(kb[b], kp + (it + b + NBUF) * TSTRIDE);
      __builtin_amdgcn_sched_barrier(0);
      const u32x4 pf = softmax_tile(s0, s1, m, l, acc);
      pv_tile(vb[b], pf, acc);
      __builtin_amdgcn_sched_barrier(0);
      load_v(vb[b], vp + (it + b + NBUF) * TSTRIDE);
      __builtin_amdgcn_sched_barrier(0);
    }
  }
#pragma unroll
  for (int b = 0; b < NBUF; ++b) {
    qk_tile(kb[b], qf, s0, s1);
    const u32x4 pf = softmax_tile(s0, s1, m, l, acc);
    pv_tile(vb[b], pf, acc);
  }

  l = xg_sum(l);
  if (c < M_) {
    float* o = lds + LDS_O + (wave * M_ + c) * LROW + 32 * g;
#pragma unroll
    for (int r = 0; r < 4; ++r) {
      *reinterpret_cast<f32x4*>(o + 8 * r) = f32x4{acc[0][r], acc[1][r], acc[2][r], acc[3][r]};
      *reinterpret_cast<f32x4*>(o + 8 * r + 4) = f32x4{acc[4][r], acc[5][r], acc[6][r], acc[7][r]};
    }
    if (g == 0) {
      lds[LDS_ML + (wave * M_ + c) * 2] = m;
      lds[LDS_ML + (wave * M_ + c) * 2 + 1] = l;
    }
  }
  __syncthreads();
  for (int idx = tid; idx < M_ * (D_ / 4); idx += NTHR) {
    const int row = idx >> 5, d4 = (idx & 31) * 4;
    float M = -INFINITY;
#pragma unroll
    for (int w = 0; w < WAVES; ++w) M = fmaxf(M, lds[LDS_ML + (w * M_ + row) * 2]);
    float L = 0.f;
    f32x4 o = {0.f, 0.f, 0.f, 0.f};
#pragma unroll
    for (int w = 0; w < WAVES; ++w) {
      const float wt = __builtin_amdgcn_exp2f((lds[LDS_ML + (w * M_ + row) * 2] - M) * C2);
      L = fmaf(wt, lds[LDS_ML + (w * M_ + row) * 2 + 1], L);
      o += wt * *reinterpret_cast<const f32x4*>(lds + LDS_O + (w * M_ + row) * LROW + d4);
    }
    const size_t slot = ((size_t)bh * NSPLIT + split) * M_ + row;
    *reinterpret_cast<f32x4*>(po + slot * D_ + d4) = o;
    if ((idx & 31) == 0) *reinterpret_cast<f32x2*>(pml + slot * 2) = f32x2{M, L};
  }
}

__device__ __forceinline__ float wave_sum(float x) {
#pragma unroll
  for (int off = 32; off >= 1; off >>= 1) x += __shfl_xor(x, off, 64);
  return x;
}

__global__ void __launch_bounds__(256)
attn_combine_kernel(const __hip_bfloat16* __restrict__ q, const __hip_bfloat16* __restrict__ kt,
                    const __hip_bfloat16* __restrict__ vt, const __hip_bfloat16* __restrict__ mask,
                    const float* __restrict__ pml, const float* __restrict__ po, __hip_bfloat16* __restrict__ out) {
  const int lane = threadIdx.x & 63;
  const int row = blockIdx.x * 4 + (threadIdx.x >> 6);
  const int t = row & 3, bhq = row >> 2, b = bhq / HQ, h = bhq % HQ;
  const int kvh = h / REP, bh = b * HKV + kvh, cq = (h % REP) * T_ + t;

  float pm[NSPLIT], pl[NSPLIT];
  f32x2 pov[NSPLIT];
#pragma unroll
  for (int s = 0; s < NSPLIT; ++s) {
    const size_t slot = ((size_t)bh * NSPLIT + s) * M_ + cq;
    const f32x2 ml = *reinterpret_cast<const f32x2*>(pml + slot * 2);
    pm[s] = ml[0];
    pl[s] = ml[1];
    pov[s] = *reinterpret_cast<const f32x2*>(po + slot * D_ + 2 * lane);
  }
  const unsigned qw = *reinterpret_cast<const unsigned*>(reinterpret_cast<const char*>(q) + ((size_t)row * D_ + 2 * lane) * 2);
  const float q0 = __uint_as_float(qw << 16), q1 = __uint_as_float(qw & 0xffff0000u);
  float z[T_], v0[T_], v1[T_];
#pragma unroll
  for (int j = 0; j < T_; ++j) {
    const size_t off = (((size_t)bh * T_ + j) * D_ + 2 * lane) * 2;
    const unsigned kw = *reinterpret_cast<const unsigned*>(reinterpret_cast<const char*>(kt) + off);
    const unsigned vw = *reinterpret_cast<const unsigned*>(reinterpret_cast<const char*>(vt) + off);
    v0[j] = __uint_as_float(vw << 16);
    v1[j] = __uint_as_float(vw & 0xffff0000u);
    float d = q0 * __uint_as_float(kw << 16) + q1 * __uint_as_float(kw & 0xffff0000u);
    d = wave_sum(d);
    const float mk = __bfloat162float(mask[(b * T_ + t) * T_ + j]);
    z[j] = (d * SCALE + (1.0f - mk) * -1000000000.0f) * LOG2E;
  }
  float M = -INFINITY;
#pragma unroll
  for (int s = 0; s < NSPLIT; ++s) { pm[s] *= C2; M = fmaxf(M, pm[s]); }
#pragma unroll
  for (int j = 0; j < T_; ++j) M = fmaxf(M, z[j]);
  float L = 0.f, o0 = 0.f, o1 = 0.f;
#pragma unroll
  for (int s = 0; s < NSPLIT; ++s) {
    const float w = __builtin_amdgcn_exp2f(pm[s] - M);
    L = fmaf(w, pl[s], L);
    o0 = fmaf(w, pov[s][0], o0);
    o1 = fmaf(w, pov[s][1], o1);
  }
#pragma unroll
  for (int j = 0; j < T_; ++j) {
    const float w = __builtin_amdgcn_exp2f(z[j] - M);
    L += w;
    o0 = fmaf(w, v0[j], o0);
    o1 = fmaf(w, v1[j], o1);
  }
  const float inv = 1.0f / L;
  *reinterpret_cast<unsigned*>(reinterpret_cast<char*>(out) + ((size_t)row * D_ + 2 * lane) * 2) = pack_bf16(o0 * inv, o1 * inv);
}

}

extern "C" void kernel_launch(void* const* d_in, const int* in_sizes, int n_in, void* d_out, int out_size, void* d_ws,
                              size_t ws_size, hipStream_t stream) {
  (void)in_sizes; (void)n_in; (void)out_size; (void)ws_size;
  const __hip_bfloat16* q = (const __hip_bfloat16*)d_in[0];
  const __hip_bfloat16* kc = (const __hip_bfloat16*)d_in[1];
  const __hip_bfloat16* vc = (const __hip_bfloat16*)d_in[2];
  const __hip_bfloat16* kt = (const __hip_bfloat16*)d_in[3];
  const __hip_bfloat16* vt = (const __hip_bfloat16*)d_in[4];
  const __hip_bfloat16* mask = (const __hip_bfloat16*)d_in[5];
  float* pml = (float*)((char*)d_ws + WS_PML);
  float* po = (float*)((char*)d_ws + WS_PO);
  hipLaunchKernelGGL(attn_split_kernel, dim3(NBH * NSPLIT), dim3(NTHR), 0, stream, q, kc, vc, pml, po);
  hipLaunchKernelGGL(attn_combine_kernel, dim3(B_ * HQ * T_ / 4), dim3(256), 0, stream, q, kt, vt, mask,
                     (const float*)pml, (const float*)po, (__hip_bfloat16*)d_out);
}
```

```cpp
#include <hip/hip_runtime.h>
#include <hip/hip_bf16.h>
#include <stdint.h>

namespace {

constexpr int B_ = 8, HQ = 32, HKV = 16, T_ = 4, C_ = 8192, D_ = 128;
constexpr int REP = HQ / HKV;
constexpr int M_ = REP * T_;
constexpr int NBH = B_ * HKV;

#define K_WAVES 2
#define K_NSPLIT 2
#define K_NT 1
#define K_CH 1
constexpr int WAVES = K_WAVES;
constexpr int NSPLIT = K_NSPLIT;
constexpr int NTHR = WAVES * 64;
constexpr int KEYS_PER_WAVE = C_ / (NSPLIT * WAVES);
constexpr int TILE = 32;
constexpr int NTILES = KEYS_PER_WAVE / TILE;
#define K_NBUF 2
constexpr int CH = K_CH;
static_assert(NTILES % CH == 0, "chunking");
constexpr int NBUF = K_NBUF;
static_assert(NTILES >= NBUF && NTILES % NBUF == 0, "tile count");
#define K_SPLIT 1
#define K_MINW 1

constexpr float SCALE = 0.088388347648318440f;
constexpr float LOG2E = 1.4426950408889634f;
constexpr float C2 = SCALE * LOG2E;

constexpr size_t WS_PML = 0;
constexpr size_t WS_PO = 65536;
constexpr size_t WS_END = WS_PO + (size_t)NBH * NSPLIT * M_ * D_ * 4;

typedef __attribute__((ext_vector_type(8))) __bf16 bf16x8;
typedef __attribute__((ext_vector_type(2))) __bf16 bf16x2;
typedef __attribute__((ext_vector_type(2))) float f32x2;
typedef __attribute__((ext_vector_type(4))) float f32x4;
typedef __attribute__((ext_vector_type(4))) unsigned int u32x4;

__device__ __forceinline__ u32x4 ld_stream(const char* p) {
  return __builtin_nontemporal_load(reinterpret_cast<const u32x4*>(p));
}

__device__ __forceinline__ f32x4 mfma16(u32x4 a, u32x4 b, f32x4 c) {
  return __builtin_amdgcn_mfma_f32_16x16x32_bf16(__builtin_bit_cast(bf16x8, a), __builtin_bit_cast(bf16x8, b), c, 0, 0, 0);
}

__device__ __forceinline__ unsigned pack_bf16(float lo, float hi) {
  f32x2 v = {lo, hi};
  return __builtin_bit_cast(unsigned, __builtin_convertvector(v, bf16x2));
}

__device__ __forceinline__ float xg_max(float x) {
  auto s = __builtin_amdgcn_permlane16_swap(__float_as_uint(x), __float_as_uint(x), false, false);
  x = fmaxf(__uint_as_float(s[0]), __uint_as_float(s[1]));
  auto t = __builtin_amdgcn_permlane32_swap(__float_as_uint(x), __float_as_uint(x), false, false);
  return fmaxf(__uint_as_float(t[0]), __uint_as_float(t[1]));
}
__device__ __forceinline__ float xg_sum(float x) {
  auto s = __builtin_amdgcn_permlane16_swap(__float_as_uint(x), __float_as_uint(x), false, false);
  x = __uint_as_float(s[0]) + __uint_as_float(s[1]);
  auto t = __builtin_amdgcn_permlane32_swap(__float_as_uint(x), __float_as_uint(x), false, false);
  return __uint_as_float(t[0]) + __uint_as_float(t[1]);
}

struct KTile { u32x4 k[8]; };
struct VTile { u32x4 v[8]; };

__device__ __forceinline__ void load_k(KTile& t, const char* kp) {
#pragma unroll
  for (int i = 0; i < 8; ++i) t.k[i] = ld_stream(kp + (i >> 2) * 4096 + (i & 3) * 64);
}
__device__ __forceinline__ void load_v(VTile& t, const char* vp) {
#pragma unroll
  for (int j = 0; j < 8; ++j) t.v[j] = ld_stream(vp + (j >> 2) * 4096 + (j & 3) * 256);
}

__device__ __forceinline__ void qk_tile(const KTile& t, const u32x4 (&qf)[4], f32x4& s0, f32x4& s1) {
  s0 = f32x4{0.f, 0.f, 0.f, 0.f};
  s1 = f32x4{0.f, 0.f, 0.f, 0.f};
#pragma unroll
  for (int ks = 0; ks < 4; ++ks) {
    s0 = mfma16(t.k[ks], qf[ks], s0);
    s1 = mfma16(t.k[4 + ks], qf[ks], s1);
  }
}

__device__ __forceinline__ u32x4 softmax_tile(const f32x4& s0, const f32x4& s1, float& m, float& l, f32x4 (&acc)[8]) {
  float mx = fmaxf(fmaxf(fmaxf(s0[0], s0[1]), fmaxf(s0[2], s0[3])), fmaxf(fmaxf(s1[0], s1[1]), fmaxf(s1[2], s1[3])));
  mx = xg_max(mx);
  if (__builtin_amdgcn_ballot_w64(mx > m) != 0ull) {
    const float mn = fmaxf(m, mx);
    const float alpha = __builtin_amdgcn_exp2f((m - mn) * C2);
    m = mn;
    l *= alpha;
#pragma unroll
    for (int i = 0; i < 8; ++i) acc[i] *= alpha;
  }
  const float mc = m * C2;
  float p0[4], p1[4];
#pragma unroll
  for (int r = 0; r < 4; ++r) {
    p0[r] = __builtin_amdgcn_exp2f(fmaf(s0[r], C2, -mc));
    p1[r] = __builtin_amdgcn_exp2f(fmaf(s1[r], C2, -mc));
  }
  l += ((p0[0] + p0[1]) + (p0[2] + p0[3])) + ((p1[0] + p1[1]) + (p1[2] + p1[3]));
  return u32x4{pack_bf16(p0[0], p0[1]), pack_bf16(p0[2], p0[3]), pack_bf16(p1[0], p1[1]), pack_bf16(p1[2], p1[3])};
}

__device__ __forceinline__ void pv_tile(const VTile& t, const u32x4& pf, f32x4 (&acc)[8]) {
#pragma unroll
  for (int i = 0; i < 8; ++i) {
    const unsigned sel = (i & 1) ? 0x07060302u : 0x05040100u;
    u32x4 vt;
#pragma unroll
    for (int w = 0; w < 4; ++w) vt[w] = __builtin_amdgcn_perm(t.v[2 * w + 1][i >> 1], t.v[2 * w][i >> 1], sel);
    acc[i] = mfma16(vt, pf, acc[i]);
  }
}

constexpr int LROW = D_ + 4;
constexpr int LDS_O = 0, LDS_ML = WAVES * M_ * LROW;
constexpr int LDS_FLOATS = LDS_ML + WAVES * M_ * 2;

__global__ void __launch_bounds__(NTHR, K_MINW)
attn_split_kernel(const __hip_bfloat16* __restrict__ q, const __hip_bfloat16* __restrict__ kc,
                  const __hip_bfloat16* __restrict__ vc, float* __restrict__ pml, float* __restrict__ po) {
  __shared__ __attribute__((aligned(16))) float lds[LDS_FLOATS];
  const int tid = threadIdx.x, lane = tid & 63;
  const int wave = __builtin_amdgcn_readfirstlane(tid >> 6);
  const int c = lane & 15, g = lane >> 4;
  const int bh = blockIdx.x / NSPLIT, split = blockIdx.x % NSPLIT;
  constexpr int GW = NSPLIT * WAVES;
  const int key0 = (split * WAVES + wave) * (TILE * CH);
  auto toff = [](int it) -> int { return (it / CH) * (TILE * 256 * CH * GW) + (it % CH) * (TILE * 256); };

  const char* kbase = reinterpret_cast<const char*>(kc) + ((size_t)bh * C_ + key0) * (D_ * 2);
  const char* vbase = reinterpret_cast<const char*>(vc) + ((size_t)bh * C_ + key0) * (D_ * 2);
  const char* kp = kbase + c * 256 + g * 16;
  const char* vp = vbase + g * 1024 + c * 16;

  u32x4 qf[4];
  {
    const char* qp = reinterpret_cast<const char*>(q) + ((size_t)bh * M_ + (c & 7)) * (D_ * 2) + g * 16;
#pragma unroll
    for (int ks = 0; ks < 4; ++ks) {
      u32x4 x = *reinterpret_cast<const u32x4*>(qp + ks * 64);
      qf[ks] = (c < M_) ? x : u32x4{0u, 0u, 0u, 0u};
    }
  }
  __builtin_amdgcn_sched_barrier(0);

  KTile kb[NBUF];
  VTile vb[NBUF];
#pragma unroll
  for (int b = 0; b < NBUF; ++b) {
    load_k(kb[b], kp + toff(b));
    __builtin_amdgcn_sched_barrier(0);
    load_v(vb[b], vp + toff(b));
    __builtin_amdgcn_sched_barrier(0);
  }

  float m = -INFINITY, l = 0.f;
  f32x4 acc[8];
#pragma unroll
  for (int i = 0; i < 8; ++i) acc[i] = f32x4{0.f, 0.f, 0.f, 0.f};

  f32x4 s0, s1;
  for (int it = 0; it < NTILES - NBUF; it += NBUF) {
#pragma unroll
    for (int b = 0; b < NBUF; ++b) {
      qk_tile(kb[b], qf, s0, s1);
      __builtin_amdgcn_sched_barrier(0);
      load_k(kb[b], kp + toff(it + b + NBUF));
      __builtin_amdgcn_sched_barrier(0);
      const u32x4 pf = softmax_tile(s0, s1, m, l, acc);
      pv_tile(vb[b], pf, acc);
      __builtin_amdgcn_sched_barrier(0);
      load_v(vb[b], vp + toff(it + b + NBUF));
      __builtin_amdgcn_sched_barrier(0);
    }
  }
#pragma unroll
  for (int b = 0; b < NBUF; ++b) {
    qk_tile(kb[b], qf, s0, s1);
    const u32x4 pf = softmax_tile(s0, s1, m, l, acc);
    pv_tile(vb[b], pf, acc);
  }

  l = xg_sum(l);
  if (c < M_) {
    float* o = lds + LDS_O + (wave * M_ + c) * LROW + 32 * g;
#pragma unroll
    for (int r = 0; r < 4; ++r) {
      *reinterpret_cast<f32x4*>(o + 8 * r) = f32x4{acc[0][r], acc[1][r], acc[2][r], acc[3][r]};
      *reinterpret_cast<f32x4*>(o + 8 * r + 4) = f32x4{acc[4][r], acc[5][r], acc[6][r], acc[7][r]};
    }
    if (g == 0) {
      lds[LDS_ML + (wave * M_ + c) * 2] = m;
      lds[LDS_ML + (wave * M_ + c) * 2 + 1] = l;
    }
  }
  __syncthreads();
  for (int idx = tid; idx < M_ * (D_ / 4); idx += NTHR) {
    const int row = idx >> 5, d4 = (idx & 31) * 4;
    float M = -INFINITY;
#pragma unroll
    for (int w = 0; w < WAVES; ++w) M = fmaxf(M, lds[LDS_ML + (w * M_ + row) * 2]);
    float L = 0.f;
    f32x4 o = {0.f, 0.f, 0.f, 0.f};
#pragma unroll
    for (int w = 0; w < WAVES; ++w) {
      const float wt = __builtin_amdgcn_exp2f((lds[LDS_ML + (w * M_ + row) * 2] - M) * C2);
      L = fmaf(wt, lds[LDS_ML + (w * M_ + row) * 2 + 1], L);
      o += wt * *reinterpret_cast<const f32x4*>(lds + LDS_O + (w * M_ + row) * LROW + d4);
    }
    const size_t slot = ((size_t)bh * NSPLIT + split) * M_ + row;
    *reinterpret_cast<f32x4*>(po + slot * D_ + d4) = o;
    if ((idx & 31) == 0) *reinterpret_cast<f32x2*>(pml + slot * 2) = f32x2{M, L};
  }
}

__device__ __forceinline__ float wave_sum(float x) {
#pragma unroll
  for (int off = 32; off >= 1; off >>= 1) x += __shfl_xor(x, off, 64);
  return x;
}

__global__ void __launch_bounds__(256)
attn_combine_kernel(const __hip_bfloat16* __restrict__ q, const __hip_bfloat16* __restrict__ kt,
                    const __hip_bfloat16* __restrict__ vt, const __hip_bfloat16* __restrict__ mask,
                    const float* __restrict__ pml, const float* __restrict__ po, __hip_bfloat16* __restrict__ out) {
  const int lane = threadIdx.x & 63;
  const int row = blockIdx.x * 4 + (threadIdx.x >> 6);
  const int t = row & 3, bhq = row >> 2, b = bhq / HQ, h = bhq % HQ;
  const int kvh = h / REP, bh = b * HKV + kvh, cq = (h % REP) * T_ + t;

  float pm[NSPLIT], pl[NSPLIT];
  f32x2 pov[NSPLIT];
#pragma unroll
  for (int s = 0; s < NSPLIT; ++s) {
    const size_t slot = ((size_t)bh * NSPLIT + s) * M_ + cq;
    const f32x2 ml = *reinterpret_cast<const f32x2*>(pml + slot * 2);
    pm[s] = ml[0];
    pl[s] = ml[1];
    pov[s] = *reinterpret_cast<const f32x2*>(po + slot * D_ + 2 * lane);
  }
  const unsigned qw = *reinterpret_cast<const unsigned*>(reinterpret_cast<const char*>(q) + ((size_t)row * D_ + 2 * lane) * 2);
  const float q0 = __uint_as_float(qw << 16), q1 = __uint_as_float(qw & 0xffff0000u);
  float z[T_], v0[T_], v1[T_];
#pragma unroll
  for (int j = 0; j < T_; ++j) {
    const size_t off = (((size_t)bh * T_ + j) * D_ + 2 * lane) * 2;
    const unsigned kw = *reinterpret_cast<const unsigned*>(reinterpret_cast<const char*>(kt) + off);
    const unsigned vw = *reinterpret_cast<const unsigned*>(reinterpret_cast<const char*>(vt) + off);
    v0[j] = __uint_as_float(vw << 16);
    v1[j] = __uint_as_float(vw & 0xffff0000u);
    float d = q0 * __uint_as_float(kw << 16) + q1 * __uint_as_float(kw & 0xffff0000u);
    d = wave_sum(d);
    const float mk = __bfloat162float(mask[(b * T_ + t) * T_ + j]);
    z[j] = (d * SCALE + (1.0f - mk) * -1000000000.0f) * LOG2E;
  }
  float M = -INFINITY;
#pragma unroll
  for (int s = 0; s < NSPLIT; ++s) { pm[s] *= C2; M = fmaxf(M, pm[s]); }
#pragma unroll
  for (int j = 0; j < T_; ++j) M = fmaxf(M, z[j]);
  float L = 0.f, o0 = 0.f, o1 = 0.f;
#pragma unroll
  for (int s = 0; s < NSPLIT; ++s) {
    const float w = __builtin_amdgcn_exp2f(pm[s] - M);
    L = fmaf(w, pl[s], L);
    o0 = fmaf(w, pov[s][0], o0);
    o1 = fmaf(w, pov[s][1], o1);
  }
#pragma unroll
  for (int j = 0; j < T_; ++j) {
    const float w = __builtin_amdgcn_exp2f(z[j] - M);
    L += w;
    o0 = fmaf(w, v0[j], o0);
    o1 = fmaf(w, v1[j], o1);
  }
  const float inv = 1.0f / L;
  *reinterpret_cast<unsigned*>(reinterpret_cast<char*>(out) + ((size_t)row * D_ + 2 * lane) * 2) = pack_bf16(o0 * inv, o1 * inv);
}

}

extern "C" void kernel_launch(void* const* d_in, const int* in_sizes, int n_in, void* d_out, int out_size, void* d_ws,
                              size_t ws_size, hipStream_t stream) {
  (void)in_sizes; (void)n_in; (void)out_size; (void)ws_size;
  const __hip_bfloat16* q = (const __hip_bfloat16*)d_in[0];
  const __hip_bfloat16* kc = (const __hip_bfloat16*)d_in[1];
  const __hip_bfloat16* vc = (const __hip_bfloat16*)d_in[2];
  const __hip_bfloat16* kt = (const __hip_bfloat16*)d_in[3];
  const __hip_bfloat16* vt = (const __hip_bfloat16*)d_in[4];
  const __hip_bfloat16* mask = (const __hip_bfloat16*)d_in[5];
  float* pml = (float*)((char*)d_ws + WS_PML);
  float* po = (float*)((char*)d_ws + WS_PO);
  hipLaunchKernelGGL(attn_split_kernel, dim3(NBH * NSPLIT), dim3(NTHR), 0, stream, q, kc, vc, pml, po);
  hipLaunchKernelGGL(attn_combine_kernel, dim3(B_ * HQ * T_ / 4), dim3(256), 0, stream, q, kt, vt, mask,
                     (const float*)pml, (const float*)po, (__hip_bfloat16*)d_out);
}
```

```cpp
#include <hip/hip_runtime.h>
#include <hip/hip_bf16.h>
#include <stdint.h>

namespace {

constexpr int B_ = 8, HQ = 32, HKV = 16, T_ = 4, C_ = 8192, D_ = 128;
constexpr int REP = HQ / HKV;
constexpr int M_ = REP * T_;
constexpr int NBH = B_ * HKV;

#define K_WAVES 2
#define K_NSPLIT 2
#define K_NT 1
#define K_CH 1
constexpr int WAVES = K_WAVES;
constexpr int NSPLIT = K_NSPLIT;
constexpr int NTHR = WAVES * 64;
constexpr int KEYS_PER_WAVE = C_ / (NSPLIT * WAVES);
constexpr int TILE = 32;
constexpr int NTILES = KEYS_PER_WAVE / TILE;
#define K_NBUF 2
constexpr int CH = K_CH;
static_assert(NTILES % CH == 0, "chunking");
constexpr int NBUF = K_NBUF;
static_assert(NTILES >= NBUF && NTILES % NBUF == 0, "tile count");
#define K_SPLIT 1
#define K_MINW 1

constexpr float SCALE = 0.088388347648318440f;
constexpr float LOG2E = 1.4426950408889634f;
constexpr float C2 = SCALE * LOG2E;

constexpr size_t WS_PML = 0;
constexpr size_t WS_PO = 65536;
constexpr size_t WS_END = WS_PO + (size_t)NBH * NSPLIT * M_ * D_ * 4;

typedef __attribute__((ext_vector_type(8))) __bf16 bf16x8;
typedef __attribute__((ext_vector_type(2))) __bf16 bf16x2;
typedef __attribute__((ext_vector_type(2))) float f32x2;
typedef __attribute__((ext_vector_type(4))) float f32x4;
typedef __attribute__((ext_vector_type(4))) unsigned int u32x4;

__device__ __forceinline__ u32x4 ld_stream(const char* p) {
  return __builtin_nontemporal_load(reinterpret_cast<const u32x4*>(p));
}

__device__ __forceinline__ f32x4 mfma16(u32x4 a, u32x4 b, f32x4 c) {
  return __builtin_amdgcn_mfma_f32_16x16x32_bf16(__builtin_bit_cast(bf16x8, a), __builtin_bit_cast(bf16x8, b), c, 0, 0, 0);
}

__device__ __forceinline__ unsigned pack_bf16(float lo, float hi) {
  f32x2 v = {lo, hi};
  return __builtin_bit_cast(unsigned, __builtin_convertvector(v, bf16x2));
}

__device__ __forceinline__ float xg_max(float x) {
  auto s = __builtin_amdgcn_permlane16_swap(__float_as_uint(x), __float_as_uint(x), false, false);
  x = fmaxf(__uint_as_float(s[0]), __uint_as_float(s[1]));
  auto t = __builtin_amdgcn_permlane32_swap(__float_as_uint(x), __float_as_uint(x), false, false);
  return fmaxf(__uint_as_float(t[0]), __uint_as_float(t[1]));
}
__device__ __forceinline__ float xg_sum(float x) {
  auto s = __builtin_amdgcn_permlane16_swap(__float_as_uint(x), __float_as_uint(x), false, false);
  x = __uint_as_float(s[0]) + __uint_as_float(s[1]);
  auto t = __builtin_amdgcn_permlane32_swap(__float_as_uint(x), __float_as_uint(x), false, false);
  return __uint_as_float(t[0]) + __uint_as_float(t[1]);
}

struct KTile { u32x4 k[8]; };
struct VTile { u32x4 v[8]; };

__device__ __forceinline__ void load_k(KTile& t, const char* kp) {
#pragma unroll
  for (int i = 0; i < 8; ++i) t.k[i] = ld_stream(kp + (i >> 2) * 4096 + (i & 3) * 64);
}
__device__ __forceinline__ void load_v(VTile& t, const char* vp) {
#pragma unroll
  for (int j = 0; j < 8; ++j) t.v[j] = ld_stream(vp + (j >> 2) * 4096 + (j & 3) * 256);
}

__device__ __forceinline__ void qk_tile(const KTile& t, const u32x4 (&qf)[4], f32x4& s0, f32x4& s1) {
  s0 = f32x4{0.f, 0.f, 0.f, 0.f};
  s1 = f32x4{0.f, 0.f, 0.f, 0.f};
#pragma unroll
  for (int ks = 0; ks < 4; ++ks) {
    s0 = mfma16(t.k[ks], qf[ks], s0);
    s1 = mfma16(t.k[4 + ks], qf[ks], s1);
  }
}

__device__ __forceinline__ u32x4 softmax_tile(const f32x4& s0, const f32x4& s1, float& m, float& l, f32x4 (&acc)[8]) {
  float mx = fmaxf(fmaxf(fmaxf(s0[0], s0[1]), fmaxf(s0[2], s0[3])), fmaxf(fmaxf(s1[0], s1[1]), fmaxf(s1[2], s1[3])));
  mx = xg_max(mx);
  if (__builtin_amdgcn_ballot_w64(mx > m) != 0ull) {
    const float mn = fmaxf(m, mx);
    const float alpha = __builtin_amdgcn_exp2f((m - mn) * C2);
    m = mn;
    l *= alpha;
#pragma unroll
    for (int i = 0; i < 8; ++i) acc[i] *= alpha;
  }
  float p0[4], p1[4];
#pragma unroll
  for (int r = 0; r < 4; ++r) {
    p0[r] = __builtin_amdgcn_exp2f((s0[r] - m) * C2);
    p1[r] = __builtin_amdgcn_exp2f((s1[r] - m) * C2);
  }
  l += ((p0[0] + p0[1]) + (p0[2] + p0[3])) + ((p1[0] + p1[1]) + (p1[2] + p1[3]));
  return u32x4{pack_bf16(p0[0], p0[1]), pack_bf16(p0[2], p0[3]), pack_bf16(p1[0], p1[1]), pack_bf16(p1[2], p1[3])};
}

__device__ __forceinline__ void pv_tile(const VTile& t, const u32x4& pf, f32x4 (&acc)[8]) {
#pragma unroll
  for (int i = 0; i < 8; ++i) {
    const unsigned sel = (i & 1) ? 0x07060302u : 0x05040100u;
    u32x4 vt;
#pragma unroll
    for (int w = 0; w < 4; ++w) vt[w] = __builtin_amdgcn_perm(t.v[2 * w + 1][i >> 1], t.v[2 * w][i >> 1], sel);
    acc[i] = mfma16(vt, pf, acc[i]);
  }
}

constexpr int LROW = D_ + 4;
constexpr int LDS_O = 0, LDS_ML = WAVES * M_ * LROW;
constexpr int LDS_FLOATS = LDS_ML + WAVES * M_ * 2;

__global__ void __launch_bounds__(NTHR, K_MINW)
attn_split_kernel(const __hip_bfloat16* __restrict__ q, const __hip_bfloat16* __restrict__ kc,
                  const __hip_bfloat16* __restrict__ vc, const __hip_bfloat16* __restrict__ kt,
                  const __hip_bfloat16* __restrict__ vt, const __hip_bfloat16* __restrict__ mask,
                  float* __restrict__ pml, float* __restrict__ po) {
  __shared__ __attribute__((aligned(16))) float lds[LDS_FLOATS];
  const int tid = threadIdx.x, lane = tid & 63;
  const int wave = __builtin_amdgcn_readfirstlane(tid >> 6);
  const int c = lane & 15, g = lane >> 4;
  const int bh = blockIdx.x / NSPLIT, split = blockIdx.x % NSPLIT;
  constexpr int GW = NSPLIT * WAVES;
  const int key0 = (split * WAVES + wave) * (TILE * CH);
  auto toff = [](int it) -> int { return (it / CH) * (TILE * 256 * CH * GW) + (it % CH) * (TILE * 256); };

  const char* kbase = reinterpret_cast<const char*>(kc) + ((size_t)bh * C_ + key0) * (D_ * 2);
  const char* vbase = reinterpret_cast<const char*>(vc) + ((size_t)bh * C_ + key0) * (D_ * 2);
  const char* kp = kbase + c * 256 + g * 16;
  const char* vp = vbase + g * 1024 + c * 16;

  u32x4 qf[4];
  {
    const char* qp = reinterpret_cast<const char*>(q) + ((size_t)bh * M_ + (c & 7)) * (D_ * 2) + g * 16;
#pragma unroll
    for (int ks = 0; ks < 4; ++ks) {
      qf[ks] = *reinterpret_cast<const u32x4*>(qp + ks * 64);
    }
  }
  const bool tree_wave = (split == 0) && (wave == 0);
  u32x4 ktf[4], vtf[4];
  f32x2 mraw = {0.f, 0.f};
  if (tree_wave) {
    const char* ktp = reinterpret_cast<const char*>(kt) + ((size_t)bh * T_ + (c & 3)) * (D_ * 2) + g * 16;
    const char* vtp = reinterpret_cast<const char*>(vt) + (size_t)bh * T_ * (D_ * 2) + c * 16;
#pragma unroll
    for (int i = 0; i < 4; ++i) {
      ktf[i] = *reinterpret_cast<const u32x4*>(ktp + i * 64);
      vtf[i] = *reinterpret_cast<const u32x4*>(vtp + i * 256);
    }
    mraw = *reinterpret_cast<const f32x2*>(reinterpret_cast<const char*>(mask) + ((bh / HKV) * T_ + (c & 3)) * (T_ * 2));
  }
  __builtin_amdgcn_sched_barrier(0);

  KTile kb[NBUF];
  VTile vb[NBUF];
#pragma unroll
  for (int b = 0; b < NBUF; ++b) {
    load_k(kb[b], kp + toff(b));
    __builtin_amdgcn_sched_barrier(0);
    load_v(vb[b], vp + toff(b));
    __builtin_amdgcn_sched_barrier(0);
  }

#pragma unroll
  for (int ks = 0; ks < 4; ++ks) qf[ks] = (c < M_) ? qf[ks] : u32x4{0u, 0u, 0u, 0u};
  float m = -INFINITY, l = 0.f;
  f32x4 acc[8];
#pragma unroll
  for (int i = 0; i < 8; ++i) acc[i] = f32x4{0.f, 0.f, 0.f, 0.f};

  f32x4 s0, s1;
  if (tree_wave) {
    s0 = f32x4{0.f, 0.f, 0.f, 0.f};
#pragma unroll
    for (int ks = 0; ks < 4; ++ks) s0 = mfma16(ktf[ks], qf[ks], s0);
    const unsigned mw0 = __float_as_uint(mraw[0]), mw1 = __float_as_uint(mraw[1]);
    const float mk[4] = {__uint_as_float(mw0 << 16), __uint_as_float(mw0 & 0xffff0000u),
                         __uint_as_float(mw1 << 16), __uint_as_float(mw1 & 0xffff0000u)};
#pragma unroll
    for (int j = 0; j < 4; ++j)
      s0[j] = (g == 0) ? fmaf(1.0f - mk[j], -1000000000.0f / SCALE, s0[j]) : -INFINITY;
    s1 = f32x4{-INFINITY, -INFINITY, -INFINITY, -INFINITY};
    const u32x4 pf = softmax_tile(s0, s1, m, l, acc);
    VTile tv;
#pragma unroll
    for (int j = 0; j < 8; ++j) tv.v[j] = vtf[j & 3];
    pv_tile(tv, pf, acc);
  }
  __builtin_amdgcn_sched_barrier(0);
  for (int it = 0; it < NTILES - NBUF; it += NBUF) {
#pragma unroll
    for (int b = 0; b < NBUF; ++b) {
      qk_tile(kb[b], qf, s0, s1);
      __builtin_amdgcn_sched_barrier(0);
      load_k(kb[b], kp + toff(it + b + NBUF));
      __builtin_amdgcn_sched_barrier(0);
      const u32x4 pf = softmax_tile(s0, s1, m, l, acc);
      pv_tile(vb[b], pf, acc);
      __builtin_amdgcn_sched_barrier(0);
      load_v(vb[b], vp + toff(it + b + NBUF));
      __builtin_amdgcn_sched_barrier(0);
    }
  }
#pragma unroll
  for (int b = 0; b < NBUF; ++b) {
    qk_tile(kb[b], qf, s0, s1);
    const u32x4 pf = softmax_tile(s0, s1, m, l, acc);
    pv_tile(vb[b], pf, acc);
  }

  l = xg_sum(l);
  if (c < M_) {
    float* o = lds + LDS_O + (wave * M_ + c) * LROW + 32 * g;
#pragma unroll
    for (int r = 0; r < 4; ++r) {
      *reinterpret_cast<f32x4*>(o + 8 * r) = f32x4{acc[0][r], acc[1][r], acc[2][r], acc[3][r]};
      *reinterpret_cast<f32x4*>(o + 8 * r + 4) = f32x4{acc[4][r], acc[5][r], acc[6][r], acc[7][r]};
    }
    if (g == 0) {
      lds[LDS_ML + (wave * M_ + c) * 2] = m;
      lds[LDS_ML + (wave * M_ + c) * 2 + 1] = l;
    }
  }
  __syncthreads();
  for (int idx = tid; idx < M_ * (D_ / 4); idx += NTHR) {
    const int row = idx >> 5, d4 = (idx & 31) * 4;
    float M = -INFINITY;
#pragma unroll
    for (int w = 0; w < WAVES; ++w) M = fmaxf(M, lds[LDS_ML + (w * M_ + row) * 2]);
    float L = 0.f;
    f32x4 o = {0.f, 0.f, 0.f, 0.f};
#pragma unroll
    for (int w = 0; w < WAVES; ++w) {
      const float wt = __builtin_amdgcn_exp2f((lds[LDS_ML + (w * M_ + row) * 2] - M) * C2);
      L = fmaf(wt, lds[LDS_ML + (w * M_ + row) * 2 + 1], L);
      o += wt * *reinterpret_cast<const f32x4*>(lds + LDS_O + (w * M_ + row) * LROW + d4);
    }
    const size_t slot = ((size_t)bh * NSPLIT + split) * M_ + row;
    *reinterpret_cast<f32x4*>(po + slot * D_ + d4) = o;
    if ((idx & 31) == 0) *reinterpret_cast<f32x2*>(pml + slot * 2) = f32x2{M, L};
  }
}

__global__ void __launch_bounds__(256)
attn_combine_kernel(const float* __restrict__ pml, const float* __restrict__ po, __hip_bfloat16* __restrict__ out) {
  const int lane = threadIdx.x & 63;
  const int row = blockIdx.x * 4 + (threadIdx.x >> 6);
  const int bh = row >> 3, cq = row & 7;
  float pm[NSPLIT], pl[NSPLIT];
  f32x2 pov[NSPLIT];
#pragma unroll
  for (int s = 0; s < NSPLIT; ++s) {
    const size_t slot = ((size_t)bh * NSPLIT + s) * M_ + cq;
    const f32x2 ml = *reinterpret_cast<const f32x2*>(pml + slot * 2);
    pm[s] = ml[0];
    pl[s] = ml[1];
    pov[s] = *reinterpret_cast<const f32x2*>(po + slot * D_ + 2 * lane);
  }
  float M = -INFINITY;
#pragma unroll
  for (int s = 0; s < NSPLIT; ++s) M = fmaxf(M, pm[s]);
  float L = 0.f, o0 = 0.f, o1 = 0.f;
#pragma unroll
  for (int s = 0; s < NSPLIT; ++s) {
    const float w = __builtin_amdgcn_exp2f((pm[s] - M) * C2);
    L = fmaf(w, pl[s], L);
    o0 = fmaf(w, pov[s][0], o0);
    o1 = fmaf(w, pov[s][1], o1);
  }
  const float inv = 1.0f / L;
  *reinterpret_cast<unsigned*>(reinterpret_cast<char*>(out) + ((size_t)row * D_ + 2 * lane) * 2) = pack_bf16(o0 * inv, o1 * inv);
}

}

extern "C" void kernel_launch(void* const* d_in, const int* in_sizes, int n_in, void* d_out, int out_size, void* d_ws,
                              size_t ws_size, hipStream_t stream) {
  (void)in_sizes; (void)n_in; (void)out_size; (void)ws_size;
  const __hip_bfloat16* q = (const __hip_bfloat16*)d_in[0];
  const __hip_bfloat16* kc = (const __hip_bfloat16*)d_in[1];
  const __hip_bfloat16* vc = (const __hip_bfloat16*)d_in[2];
  const __hip_bfloat16* kt = (const __hip_bfloat16*)d_in[3];
  const __hip_bfloat16* vt = (const __hip_bfloat16*)d_in[4];
  const __hip_bfloat16* mask = (const __hip_bfloat16*)d_in[5];
  float* pml = (float*)((char*)d_ws + WS_PML);
  float* po = (float*)((char*)d_ws + WS_PO);
  hipLaunchKernelGGL(attn_split_kernel, dim3(NBH * NSPLIT), dim3(NTHR), 0, stream, q, kc, vc, kt, vt, mask, pml, po);
  hipLaunchKernelGGL(attn_combine_kernel, dim3(B_ * HQ * T_ / 4), dim3(256), 0, stream,
                     (const float*)pml, (const float*)po, (__hip_bfloat16*)d_out);
}
```

```cpp
#include <hip/hip_runtime.h>
#include <hip/hip_bf16.h>
#include <stdint.h>

namespace {

constexpr int B_ = 8, HQ = 32, HKV = 16, T_ = 4, C_ = 8192, D_ = 128;
constexpr int REP = HQ / HKV;
constexpr int M_ = REP * T_;
constexpr int NBH = B_ * HKV;

constexpr int WAVES = 2;
constexpr int NSPLIT = 2;
constexpr int GW = NSPLIT * WAVES;
constexpr int NTHR = WAVES * 64;
constexpr int TILE = 32;
constexpr int NTILES = C_ / (GW * TILE);
constexpr int NBUF = 2;
constexpr int TSTEP = TILE * D_ * 2 * GW;
static_assert(NTILES >= NBUF && NTILES % NBUF == 0, "tile count");

constexpr float SCALE = 0.088388347648318440f;
constexpr float LOG2E = 1.4426950408889634f;
constexpr float C2 = SCALE * LOG2E;

constexpr size_t WS_PML = 0;
constexpr size_t WS_PO = 65536;

typedef __attribute__((ext_vector_type(8))) __bf16 bf16x8;
typedef __attribute__((ext_vector_type(2))) __bf16 bf16x2;
typedef __attribute__((ext_vector_type(2))) float f32x2;
typedef __attribute__((ext_vector_type(4))) float f32x4;
typedef __attribute__((ext_vector_type(4))) unsigned int u32x4;

__device__ __forceinline__ u32x4 ld_stream(const char* p) {
  return __builtin_nontemporal_load(reinterpret_cast<const u32x4*>(p));
}

__device__ __forceinline__ f32x4 mfma16(u32x4 a, u32x4 b, f32x4 c) {
  return __builtin_amdgcn_mfma_f32_16x16x32_bf16(__builtin_bit_cast(bf16x8, a), __builtin_bit_cast(bf16x8, b), c, 0, 0, 0);
}

__device__ __forceinline__ unsigned pack_bf16(float lo, float hi) {
  f32x2 v = {lo, hi};
  return __builtin_bit_cast(unsigned, __builtin_convertvector(v, bf16x2));
}

__device__ __forceinline__ float xg_max(float x) {
  auto s = __builtin_amdgcn_permlane16_swap(__float_as_uint(x), __float_as_uint(x), false, false);
  x = fmaxf(__uint_as_float(s[0]), __uint_as_float(s[1]));
  auto t = __builtin_amdgcn_permlane32_swap(__float_as_uint(x), __float_as_uint(x), false, false);
  return fmaxf(__uint_as_float(t[0]), __uint_as_float(t[1]));
}
__device__ __forceinline__ float xg_sum(float x) {
  auto s = __builtin_amdgcn_permlane16_swap(__float_as_uint(x), __float_as_uint(x), false, false);
  x = __uint_as_float(s[0]) + __uint_as_float(s[1]);
  auto t = __builtin_amdgcn_permlane32_swap(__float_as_uint(x), __float_as_uint(x), false, false);
  return __uint_as_float(t[0]) + __uint_as_float(t[1]);
}

struct KTile { u32x4 k[8]; };
struct VTile { u32x4 v[8]; };

__device__ __forceinline__ void load_k(KTile& t, const char* kp) {
#pragma unroll
  for (int i = 0; i < 8; ++i) t.k[i] = ld_stream(kp + (i >> 2) * 4096 + (i & 3) * 64);
}
__device__ __forceinline__ void load_v(VTile& t, const char* vp) {
#pragma unroll
  for (int j = 0; j < 8; ++j) t.v[j] = ld_stream(vp + (j >> 2) * 4096 + (j & 3) * 256);
}

__device__ __forceinline__ void qk_tile(const KTile& t, const u32x4 (&qf)[4], f32x4& s0, f32x4& s1) {
  s0 = f32x4{0.f, 0.f, 0.f, 0.f};
  s1 = f32x4{0.f, 0.f, 0.f, 0.f};
#pragma unroll
  for (int ks = 0; ks < 4; ++ks) {
    s0 = mfma16(t.k[ks], qf[ks], s0);
    s1 = mfma16(t.k[4 + ks], qf[ks], s1);
  }
}

__device__ __forceinline__ u32x4 softmax_tile(const f32x4& s0, const f32x4& s1, float& m, float& l, f32x4 (&acc)[8]) {
  float mx = fmaxf(fmaxf(fmaxf(s0[0], s0[1]), fmaxf(s0[2], s0[3])), fmaxf(fmaxf(s1[0], s1[1]), fmaxf(s1[2], s1[3])));
  mx = xg_max(mx);
  if (__builtin_amdgcn_ballot_w64(mx > m) != 0ull) {
    const float mn = fmaxf(m, mx);
    const float alpha = __builtin_amdgcn_exp2f((m - mn) * C2);
    m = mn;
    l *= alpha;
#pragma unroll
    for (int i = 0; i < 8; ++i) acc[i] *= alpha;
  }
  float p0[4], p1[4];
#pragma unroll
  for (int r = 0; r < 4; ++r) {
    p0[r] = __builtin_amdgcn_exp2f((s0[r] - m) * C2);
    p1[r] = __builtin_amdgcn_exp2f((s1[r] - m) * C2);
  }
  l += ((p0[0] + p0[1]) + (p0[2] + p0[3])) + ((p1[0] + p1[1]) + (p1[2] + p1[3]));
  return u32x4{pack_bf16(p0[0], p0[1]), pack_bf16(p0[2], p0[3]), pack_bf16(p1[0], p1[1]), pack_bf16(p1[2], p1[3])};
}

__device__ __forceinline__ void pv_tile(const VTile& t, const u32x4& pf, f32x4 (&acc)[8]) {
#pragma unroll
  for (int i = 0; i < 8; ++i) {
    const unsigned sel = (i & 1) ? 0x07060302u : 0x05040100u;
    u32x4 vt;
#pragma unroll
    for (int w = 0; w < 4; ++w) vt[w] = __builtin_amdgcn_perm(t.v[2 * w + 1][i >> 1], t.v[2 * w][i >> 1], sel);
    acc[i] = mfma16(vt, pf, acc[i]);
  }
}

constexpr int LROW = D_ + 4;
constexpr int LDS_O = 0, LDS_ML = WAVES * M_ * LROW;
constexpr int LDS_FLOATS = LDS_ML + WAVES * M_ * 2;

__global__ void __launch_bounds__(NTHR, 1)
attn_split_kernel(const __hip_bfloat16* __restrict__ q, const __hip_bfloat16* __restrict__ kc,
                  const __hip_bfloat16* __restrict__ vc, const __hip_bfloat16* __restrict__ kt,
                  const __hip_bfloat16* __restrict__ vt, const __hip_bfloat16* __restrict__ mask,
                  float* __restrict__ pml, float* __restrict__ po) {
  __shared__ __attribute__((aligned(16))) float lds[LDS_FLOATS];
  const int tid = threadIdx.x, lane = tid & 63;
  const int wave = __builtin_amdgcn_readfirstlane(tid >> 6);
  const int c = lane & 15, g = lane >> 4;
  const int bh = blockIdx.x / NSPLIT, split = blockIdx.x % NSPLIT;
  const int key0 = (split * WAVES + wave) * TILE;
  const char* kp = reinterpret_cast<const char*>(kc) + ((size_t)bh * C_ + key0) * (D_ * 2) + c * 256 + g * 16;
  const char* vp = reinterpret_cast<const char*>(vc) + ((size_t)bh * C_ + key0) * (D_ * 2) + g * 1024 + c * 16;

  u32x4 qf[4];
  {
    const char* qp = reinterpret_cast<const char*>(q) + ((size_t)bh * M_ + (c & 7)) * (D_ * 2) + g * 16;
#pragma unroll
    for (int ks = 0; ks < 4; ++ks) qf[ks] = *reinterpret_cast<const u32x4*>(qp + ks * 64);
  }
  const bool tree_wave = (split == 0) && (wave == 0);
  u32x4 ktf[4], vtf[4];
  f32x2 mraw = {0.f, 0.f};
  if (tree_wave) {
    const char* ktp = reinterpret_cast<const char*>(kt) + ((size_t)bh * T_ + (c & 3)) * (D_ * 2) + g * 16;
    const char* vtp = reinterpret_cast<const char*>(vt) + (size_t)bh * T_ * (D_ * 2) + c * 16;
#pragma unroll
    for (int i = 0; i < 4; ++i) {
      ktf[i] = *reinterpret_cast<const u32x4*>(ktp + i * 64);
      vtf[i] = *reinterpret_cast<const u32x4*>(vtp + i * 256);
    }
    mraw = *reinterpret_cast<const f32x2*>(reinterpret_cast<const char*>(mask) + ((bh / HKV) * T_ + (c & 3)) * (T_ * 2));
  }
  __builtin_amdgcn_sched_barrier(0);

  KTile kb[NBUF];
  VTile vb[NBUF];
#pragma unroll
  for (int b = 0; b < NBUF; ++b) {
    load_k(kb[b], kp + b * TSTEP);
    __builtin_amdgcn_sched_barrier(0);
    load_v(vb[b], vp + b * TSTEP);
    __builtin_amdgcn_sched_barrier(0);
  }

#pragma unroll
  for (int ks = 0; ks < 4; ++ks) qf[ks] = (c < M_) ? qf[ks] : u32x4{0u, 0u, 0u, 0u};
  float m = -INFINITY, l = 0.f;
  f32x4 acc[8];
#pragma unroll
  for (int i = 0; i < 8; ++i) acc[i] = f32x4{0.f, 0.f, 0.f, 0.f};

  f32x4 s0, s1;
  if (tree_wave) {
    s0 = f32x4{0.f, 0.f, 0.f, 0.f};
#pragma unroll
    for (int ks = 0; ks < 4; ++ks) s0 = mfma16(ktf[ks], qf[ks], s0);
    const unsigned mw0 = __float_as_uint(mraw[0]), mw1 = __float_as_uint(mraw[1]);
    const float mk[4] = {__uint_as_float(mw0 << 16), __uint_as_float(mw0 & 0xffff0000u),
                         __uint_as_float(mw1 << 16), __uint_as_float(mw1 & 0xffff0000u)};
#pragma unroll
    for (int j = 0; j < 4; ++j)
      s0[j] = (g == 0) ? fmaf(1.0f - mk[j], -1000000000.0f / SCALE, s0[j]) : -INFINITY;
    s1 = f32x4{-INFINITY, -INFINITY, -INFINITY, -INFINITY};
    const u32x4 pf = softmax_tile(s0, s1, m, l, acc);
    VTile tv;
#pragma unroll
    for (int j = 0; j < 8; ++j) tv.v[j] = vtf[j & 3];
    pv_tile(tv, pf, acc);
  }
  __builtin_amdgcn_sched_barrier(0);

  for (int it = 0; it < NTILES - NBUF; it += NBUF) {
#pragma unroll
    for (int b = 0; b < NBUF; ++b) {
      qk_tile(kb[b], qf, s0, s1);
      __builtin_amdgcn_sched_barrier(0);
      load_k(kb[b], kp + (it + b + NBUF) * TSTEP);
      __builtin_amdgcn_sched_barrier(0);
      const u32x4 pf = softmax_tile(s0, s1, m, l, acc);
      pv_tile(vb[b], pf, acc);
      __builtin_amdgcn_sched_barrier(0);
      load_v(vb[b], vp + (it + b + NBUF) * TSTEP);
      __builtin_amdgcn_sched_barrier(0);
    }
  }
#pragma unroll
  for (int b = 0; b < NBUF; ++b) {
    qk_tile(kb[b], qf, s0, s1);
    const u32x4 pf = softmax_tile(s0, s1, m, l, acc);
    pv_tile(vb[b], pf, acc);
  }

  l = xg_sum(l);
  if (c < M_) {
    float* o = lds + LDS_O + (wave * M_ + c) * LROW + 32 * g;
#pragma unroll
    for (int r = 0; r < 4; ++r) {
      *reinterpret_cast<f32x4*>(o + 8 * r) = f32x4{acc[0][r], acc[1][r], acc[2][r], acc[3][r]};
      *reinterpret_cast<f32x4*>(o + 8 * r + 4) = f32x4{acc[4][r], acc[5][r], acc[6][r], acc[7][r]};
    }
    if (g == 0) {
      lds[LDS_ML + (wave * M_ + c) * 2] = m;
      lds[LDS_ML + (wave * M_ + c) * 2 + 1] = l;
    }
  }
  __syncthreads();
  for (int idx = tid; idx < M_ * (D_ / 4); idx += NTHR) {
    const int row = idx >> 5, d4 = (idx & 31) * 4;
    float M = -INFINITY;
#pragma unroll
    for (int w = 0; w < WAVES; ++w) M = fmaxf(M, lds[LDS_ML + (w * M_ + row) * 2]);
    float L = 0.f;
    f32x4 o = {0.f, 0.f, 0.f, 0.f};
#pragma unroll
    for (int w = 0; w < WAVES; ++w) {
      const float wt = __builtin_amdgcn_exp2f((lds[LDS_ML + (w * M_ + row) * 2] - M) * C2);
      L = fmaf(wt, lds[LDS_ML + (w * M_ + row) * 2 + 1], L);
      o += wt * *reinterpret_cast<const f32x4*>(lds + LDS_O + (w * M_ + row) * LROW + d4);
    }
    const size_t slot = ((size_t)bh * NSPLIT + split) * M_ + row;
    *reinterpret_cast<f32x4*>(po + slot * D_ + d4) = o;
    if ((idx & 31) == 0) *reinterpret_cast<f32x2*>(pml + slot * 2) = f32x2{M, L};
  }
}

__global__ void __launch_bounds__(256)
attn_combine_kernel(const float* __restrict__ pml, const float* __restrict__ po, __hip_bfloat16* __restrict__ out) {
  const int lane = threadIdx.x & 63;
  const int row = blockIdx.x * 4 + (threadIdx.x >> 6);
  const int bh = row >> 3, cq = row & 7;
  float pm[NSPLIT], pl[NSPLIT];
  f32x2 pov[NSPLIT];
#pragma unroll
  for (int s = 0; s < NSPLIT; ++s) {
    const size_t slot = ((size_t)bh * NSPLIT + s) * M_ + cq;
    const f32x2 ml = *reinterpret_cast<const f32x2*>(pml + slot * 2);
    pm[s] = ml[0];
    pl[s] = ml[1];
    pov[s] = *reinterpret_cast<const f32x2*>(po + slot * D_ + 2 * lane);
  }
  float M = -INFINITY;
#pragma unroll
  for (int s = 0; s < NSPLIT; ++s) M = fmaxf(M, pm[s]);
  float L = 0.f, o0 = 0.f, o1 = 0.f;
#pragma unroll
  for (int s = 0; s < NSPLIT; ++s) {
    const float w = __builtin_amdgcn_exp2f((pm[s] - M) * C2);
    L = fmaf(w, pl[s], L);
    o0 = fmaf(w, pov[s][0], o0);
    o1 = fmaf(w, pov[s][1], o1);
  }
  const float inv = 1.0f / L;
  *reinterpret_cast<unsigned*>(reinterpret_cast<char*>(out) + ((size_t)row * D_ + 2 * lane) * 2) = pack_bf16(o0 * inv, o1 * inv);
}

}

extern "C" void kernel_launch(void* const* d_in, const int* in_sizes, int n_in, void* d_out, int out_size, void* d_ws,
                              size_t ws_size, hipStream_t stream) {
  (void)in_sizes; (void)n_in; (void)out_size; (void)ws_size;
  const __hip_bfloat16* q = (const __hip_bfloat16*)d_in[0];
  const __hip_bfloat16* kc = (const __hip_bfloat16*)d_in[1];
  const __hip_bfloat16* vc = (const __hip_bfloat16*)d_in[2];
  const __hip_bfloat16* kt = (const __hip_bfloat16*)d_in[3];
  const __hip_bfloat16* vt = (const __hip_bfloat16*)d_in[4];
  const __hip_bfloat16* mask = (const __hip_bfloat16*)d_in[5];
  float* pml = (float*)((char*)d_ws + WS_PML);
  float* po = (float*)((char*)d_ws + WS_PO);
  hipLaunchKernelGGL(attn_split_kernel, dim3(NBH * NSPLIT), dim3(NTHR), 0, stream, q, kc, vc, kt, vt, mask, pml, po);
  hipLaunchKernelGGL(attn_combine_kernel, dim3(B_ * HQ * T_ / 4), dim3(256), 0, stream, (const float*)pml,
                     (const float*)po, (__hip_bfloat16*)d_out);
}
```
